```python
import torch

def _softmax(x, axis=-1):
    return torch.softmax(x, dim=axis)
def _gelu(x):
    return torch.nn.functional.gelu(x, approximate="tanh")
def _silu(x):
    return torch.nn.functional.silu(x)
def _sigmoid(x):
    return torch.sigmoid(x)
def _layernorm(x, axis=-1, eps=1e-5):
    m = x.mean(dim=axis, keepdim=True)
    v = x.var(dim=axis, keepdim=True, unbiased=False)
    return (x - m) / torch.sqrt(v + eps)
def _scatter_add(tgt, idx, src, axis=0):
    return tgt.clone().index_add_(axis, idx, src)
def _segment_sum(src, seg, n):
    out = torch.zeros((n,) + src.shape[1:], dtype=src.dtype)
    return out.index_add_(0, seg, src)
def _pad(x, pw):
    flat = tuple(v for pair in reversed(pw) for v in pair)
    return torch.nn.functional.pad(x, flat)

kernel_name = 'sequential_preLN__linear_attn_block__geglu_ffn__prompt'

def setup_inputs():

    _w = (lambda a: (lambda t: t.to(torch.bfloat16) if t.is_floating_point() else t)(torch.as_tensor(a)))
    def _A_setup():
        B, S, D = 1, 2048, 640
        return {
            "x": _w((lambda *s: torch.randn(*s, dtype=torch.bfloat16) * 0.05)(B, S, D)),
            "wqkv": _w((lambda *s: torch.randn(*s, dtype=torch.bfloat16) * 0.05)(D, 3 * D)),
            "wo": _w((lambda *s: torch.randn(*s, dtype=torch.bfloat16) * 0.05)(D, D)),
        }
    _A_in = _A_setup()

    def _B_setup():
        B, S, D, F = 1, 2048, 640, 1728
        return {
            "x": _w((lambda *s: torch.randn(*s, dtype=torch.bfloat16) * 0.05)(B, S, D)),
            "w_gate": _w((lambda *s: torch.randn(*s, dtype=torch.bfloat16) * 0.05)(D, F)),
            "w_up": _w((lambda *s: torch.randn(*s, dtype=torch.bfloat16) * 0.05)(D, F)),
            "w_down": _w((lambda *s: torch.randn(*s, dtype=torch.bfloat16) * 0.05)(F, D)),
        }
    _B_in = _B_setup()

    return {
        "A_x": _A_in["x"],
        "A_wqkv": _A_in["wqkv"],
        "A_wo": _A_in["wo"],
        "B_x": _B_in["x"],
        "B_w_gate": _B_in["w_gate"],
        "B_w_up": _B_in["w_up"],
        "B_w_down": _B_in["w_down"],
    }

def reference(A_x, A_wqkv, A_wo, B_x, B_w_gate, B_w_up, B_w_down):
    def _A_ref(x, wqkv, wo):
        B, S, D = x.shape
        H = 32; Dh = D // H
        qkv = (lambda x, *d: x.permute(*d))((x @ wqkv).reshape(B, S, 3, H, Dh), 2, 0, 3, 1, 4)
        q, k, v = qkv[0], qkv[1], qkv[2]
        phi = lambda t: torch.where(t > 0, t + 1, torch.exp(t)) + 1e-6
        pq, pk = phi(q), phi(k)
        kv = torch.einsum("bhsd,bhse->bhde", pk, v)
        z = torch.einsum("bhsd->bhd", pk)
        num = torch.einsum("bhqd,bhde->bhqe", pq, kv)
        den = torch.einsum("bhqd,bhd->bhq", pq, z)[..., None] + 1e-6
        o = num / den
        return (lambda x, *d: x.permute(*d))(o, 0, 2, 1, 3).reshape(B, S, D) @ wo

    def _B_ref(x, w_gate, w_up, w_down):
        return (_gelu(x @ w_gate) * (x @ w_up)) @ w_down

    x = A_x
    h = x + _A_ref(_layernorm(x, axis=-1), A_wqkv, A_wo)
    h2 = h + _B_ref(_layernorm(h, axis=-1), B_w_gate, B_w_up, B_w_down)
    return h2
```

```cpp
#include <hip/hip_runtime.h>
#include <stdint.h>
#include <type_traits>
#pragma clang diagnostic ignored "-Winline-asm"

#define FUSED 1

#define GAS __attribute__((address_space(1)))
#define LAS __attribute__((address_space(3)))
#define DI __device__ __forceinline__
typedef unsigned v4u __attribute__((ext_vector_type(4)));
typedef unsigned v2u __attribute__((ext_vector_type(2)));
typedef float f4 __attribute__((ext_vector_type(4)));
typedef float f2 __attribute__((ext_vector_type(2)));
typedef float f16v __attribute__((ext_vector_type(16)));
typedef short s4v __attribute__((ext_vector_type(4)));
typedef __bf16 b8v __attribute__((ext_vector_type(8)));
typedef __bf16 b2v __attribute__((ext_vector_type(2)));
typedef __amdgpu_buffer_rsrc_t rsrc_t;
typedef GAS unsigned long long gu64;

constexpr int SEQ = 2048, DM = 640, FF = 1728;
constexpr unsigned WS_EPOCH = 0, WS_F1 = 4096, WS_F2 = WS_F1 + 32768, WS_F3 = WS_F2 + 32768, WS_F4 = WS_F3 + 32768;
constexpr unsigned WS_KVP = 262144;
constexpr unsigned WS_OFR = WS_KVP + 16 * 16 * 8 * 1024;
constexpr unsigned WS_HFR = WS_OFR + 64 * 40 * 1024;
constexpr unsigned WS_HST = WS_HFR + 64 * 40 * 1024;
constexpr unsigned WS_AFR = WS_HST + 10 * 2048 * 8;
constexpr unsigned WS_PQ  = WS_AFR + 64 * 108 * 1024;
constexpr unsigned WS_END = WS_PQ + 256 * 8 * 3 * 512;
constexpr unsigned LDS_BYTES = 163840;

struct Params {
  const void* x; const void* wqkv; const void* wo; const void* wg; const void* wu; const void* wd; void* out; char* ws;
};

DI f4 mfma16(v4u a, v4u b, f4 c) { return __builtin_amdgcn_mfma_f32_16x16x32_bf16(__builtin_bit_cast(b8v, a), __builtin_bit_cast(b8v, b), c, 0, 0, 0); }
DI f4 mfma16k(v2u a, v2u b, f4 c) { return __builtin_amdgcn_mfma_f32_16x16x16bf16_1k(__builtin_bit_cast(s4v, a), __builtin_bit_cast(s4v, b), c, 0, 0, 0); }
DI f16v mfma32(v4u a, v4u b, f16v c) { return __builtin_amdgcn_mfma_f32_32x32x16_bf16(__builtin_bit_cast(b8v, a), __builtin_bit_cast(b8v, b), c, 0, 0, 0); }
DI unsigned pack2(float lo, float hi) { b2v v = {(__bf16)lo, (__bf16)hi}; return __builtin_bit_cast(unsigned, v); }
DI float bfr(float x) { return (float)(__bf16)x; }
DI float bflo(unsigned u) { return __builtin_bit_cast(float, u << 16); }
DI float bfhi(unsigned u) { return __builtin_bit_cast(float, u & 0xffff0000u); }
DI rsrc_t mkrs(const void* p, unsigned bytes) { return __builtin_amdgcn_make_buffer_rsrc((void*)p, 0, bytes, 0x00020000); }
DI v2u trread(LAS char* p) { return __builtin_bit_cast(v2u, __builtin_amdgcn_ds_read_tr16_b64_v4i16((LAS s4v*)p)); }
DI v4u trfrag(LAS char* p, int toff) { v2u a = trread(p), b = trread(p + toff); return v4u{a.x, a.y, b.x, b.y}; }
DI void glds(unsigned m0v, unsigned voff, rsrc_t rs, unsigned soff) {
  asm volatile("s_mov_b32 m0, %0\n\ts_nop 0\n\tbuffer_load_dwordx4 %1, %2, %3 offen lds" ::"s"(m0v), "v"(voff), "s"(rs), "s"(soff) : "m0", "memory");
}
DI void ld_sc1(v4u& dst, unsigned voff, rsrc_t rs, unsigned soff) {
  asm volatile("buffer_load_dwordx4 %0, %1, %2, %3 offen sc1" : "=v"(dst) : "v"(voff), "s"(rs), "s"(soff) : "memory");
}
DI void ld8_sc1(f2& dst, unsigned voff, rsrc_t rs, unsigned soff) {
  asm volatile("buffer_load_dwordx2 %0, %1, %2, %3 offen sc1" : "=v"(dst) : "v"(voff), "s"(rs), "s"(soff) : "memory");
}
#define LD_SC1(dst, voff, rs, soff) ld_sc1(dst, voff, rs, soff)
#define WAITV(n) asm volatile("s_waitcnt vmcnt(%0)" ::"n"(n) : "memory")
template <int N> DI void waitv_on(v4u& r) { asm volatile("s_waitcnt vmcnt(%1)" : "+v"(r) : "n"(N) : "memory"); }
template <int I, int E, class Fn> DI void sfor(Fn&& f) {
  if constexpr (I < E) { f(std::integral_constant<int, I>{}); sfor<I + 1, E>(f); }
}
#define WAITL0() asm volatile("s_waitcnt lgkmcnt(0)" ::: "memory")
#define BAR() asm volatile("s_waitcnt lgkmcnt(0)\n\ts_barrier" ::: "memory")
constexpr int AUX_SC1 = 16;
DI void st16_sc1(v4u v, rsrc_t rs, unsigned voff, unsigned soff) { __builtin_amdgcn_raw_buffer_store_b128(v, rs, voff, soff, AUX_SC1); }
DI void st8_sc1(v2u v, rsrc_t rs, unsigned voff, unsigned soff) { __builtin_amdgcn_raw_buffer_store_b64(v, rs, voff, soff, AUX_SC1); }

DI float phi(float t) { t = bfr(t); return ((t > 0.f) ? (t + 1.f) : __expf(t)) + 1e-6f; }

DI void set_flag(char* ws, unsigned off, unsigned long long tag) {
  __hip_atomic_store((gu64*)(ws + off), tag, __ATOMIC_RELAXED, __HIP_MEMORY_SCOPE_AGENT);
}
DI void poll_flags(char* ws, unsigned off, unsigned long long tag) {
  for (unsigned spins = 0; spins < (1u << 20); ++spins) {
    unsigned long long v = tag;
    if (off != 0xffffffffu) v = __hip_atomic_load((gu64*)(ws + off), __ATOMIC_RELAXED, __HIP_MEMORY_SCOPE_AGENT);
    if (__all(v == tag)) break;
    __builtin_amdgcn_s_sleep(1);
  }
  __builtin_amdgcn_fence(__ATOMIC_ACQUIRE, "wavefront");
}

DI unsigned img128_voff(int l, int rowstride) { int r = l >> 3, c = (l & 7) ^ (((r >> 1) & 1) << 2); return r * rowstride + c * 16; }
DI unsigned img128_lane(int l, int ct) {
  int hh = l >> 5, blk = (l >> 4) & 1, q = (l & 15) >> 2, p = l & 3;
  return (4 * hh + q) * 128 + 64 * (ct ^ (q >> 1)) + 32 * blk + 8 * p;
}

template <int PH>
__global__ void __launch_bounds__(512, 2) blk_kernel(Params p) {
  extern __shared__ __attribute__((aligned(16))) char smem[];
  LAS char* lds = (LAS char*)smem;
  const unsigned ldsb = (unsigned)(size_t)lds;
  const int tid = threadIdx.x, l = tid & 63;
  const int wave = __builtin_amdgcn_readfirstlane(tid >> 6);
  const int bid = blockIdx.x, x8 = bid & 7, slot = bid >> 3;
  char* ws = p.ws;
  const rsrc_t rws = mkrs(ws, WS_END);
  constexpr bool FU = (PH == 31);
  unsigned long long tag = 0;
  if constexpr (FU) {
    tag = __hip_atomic_load((gu64*)(ws + WS_EPOCH), __ATOMIC_RELAXED, __HIP_MEMORY_SCOPE_AGENT) + 1ull;
  }

  const int T = 4 * (x8 >> 1) + (slot & 3), hp = 8 * (x8 & 1) + (slot >> 2);
  const int wstart = (x8 * 55 + 1) >> 1, wcnt = 28 - (x8 & 1);
  const bool wact = slot < wcnt;
  const int wtask = wstart + slot, wt = wtask / 10, wcu = wtask % 10;
  const int gug = x8 & 3, gtt = 4 * (x8 >> 2) + (slot & 3), gu = 7 * gug + (slot >> 2);
  const bool gact = (slot >> 2) < (gug == 3 ? 6 : 7);

  const int g4 = l >> 4, r16 = l & 15, q4 = r16 >> 2, p4 = l & 3;
  v2u pq[3];
  v4u hkeep[4];

  if constexpr (PH & 1) {
    const rsrc_t rx = mkrs(p.x, SEQ * DM * 2), rw = mkrs(p.wqkv, DM * 3 * DM * 2);
    constexpr unsigned XREG = 0, WREG = 81920, QIMG = 20480;
    const unsigned vx0 = (l >> 3) * 1280 + (((l & 7) ^ (l >> 4)) * 16);
    const unsigned vx1 = (8 + (l >> 3)) * 1280 + (((l & 7) ^ (4 + (l >> 4))) * 16);
    const unsigned xs = (128 * T + 16 * wave) * 1280;
    const unsigned xm = ldsb + XREG + wave * 10240;
    auto issue_x = [&](int hf) {
#pragma unroll
      for (int cc = 0; cc < 5; ++cc) {
        glds(xm + (cc * 2) * 1024, vx0, rx, xs + hf * 640 + cc * 128);
        glds(xm + (cc * 2 + 1) * 1024, vx1, rx, xs + hf * 640 + cc * 128);
      }
    };
    unsigned vkv[5];
#pragma unroll
    for (int i = 0; i < 5; ++i) {
      int sg = 64 * i + l, pos = sg / 10, c = sg % 10;
      int row = (pos & 19) | ((pos & 4) << 1) | ((pos & 8) >> 1);
      vkv[i] = row * 3840 + (c < 5 ? 1280 + 16 * c : 2560 + 16 * (c - 5));
    }
    auto issue_kv = [&](int s, unsigned base) {
#pragma unroll
      for (int i = 0; i < 5; ++i) glds(ldsb + base + i * 1024, vkv[i], rw, s * 122880 + 80 * hp);
    };
    issue_x(0);
    issue_kv(wave, WREG + wave * 5120);
    v4u xf[20];
    const int fx = (r16 >> 1) & 7;
    LAS char* xa0 = lds + XREG + wave * 10240 + (r16 >> 3) * 1024 + (r16 & 7) * 128 + ((g4 ^ fx) * 16);
    LAS char* xa1 = lds + XREG + wave * 10240 + (r16 >> 3) * 1024 + (r16 & 7) * 128 + ((g4 ^ fx ^ 4) * 16);
    WAITV(5);
#pragma unroll
    for (int s = 0; s < 10; ++s) xf[s] = *(LAS v4u*)(((s & 1) ? xa1 : xa0) + (s >> 1) * 2048);
    WAITL0();
    issue_x(1);
    issue_kv(wave + 8, WREG + (wave + 8) * 5120);
    const v4u ones = {0x3f803f80u, 0x3f803f80u, 0x3f803f80u, 0x3f803f80u};
    f4 gram = {0, 0, 0, 0}, rsum = {0, 0, 0, 0};
#pragma unroll
    for (int s = 0; s < 10; ++s) { gram = mfma16(xf[s], xf[s], gram); rsum = mfma16(xf[s], ones, rsum); }
    WAITV(5);
#pragma unroll
    for (int s = 0; s < 10; ++s) xf[10 + s] = *(LAS v4u*)(((s & 1) ? xa1 : xa0) + (s >> 1) * 2048);
    BAR();
    if (wave < 4) issue_kv(16 + wave, XREG + wave * 5120);
    {
      unsigned vq[3];
#pragma unroll
      for (int i = 0; i < 3; ++i) {
        int sg = 64 * i + l, pos = sg / 6, c = sg % 6;
        if (c > 4) c = 4;
        int row = (pos & 19) | ((pos & 4) << 1) | ((pos & 8) >> 1);
        vq[i] = row * 3840 + 16 * c;
      }
      auto issue_q = [&](int s) {
#pragma unroll
        for (int i = 0; i < 3; ++i) glds(ldsb + QIMG + s * 3072 + i * 1024, vq[i], rw, s * 122880 + 80 * hp);
      };
      if (wave < 4) { issue_q(2 * wave); issue_q(2 * wave + 1); }
      else { issue_q(8 + 3 * (wave - 4)); issue_q(9 + 3 * (wave - 4)); issue_q(10 + 3 * (wave - 4)); }
    }
#pragma unroll
    for (int s = 10; s < 20; ++s) { gram = mfma16(xf[s], xf[s], gram); rsum = mfma16(xf[s], ones, rsum); }
    {
      const int sel = l & 3;
      float gd = sel == 0 ? gram[0] : sel == 1 ? gram[1] : sel == 2 ? gram[2] : gram[3];
      float sd = sel == 0 ? rsum[0] : sel == 1 ? rsum[1] : sel == 2 ? rsum[2] : rsum[3];
      const int src = r16 + 16 * (r16 >> 2);
      gd = __shfl(gd, src); sd = __shfl(sd, src);
      const float mean = sd * (1.f / 640.f);
      const float var = fmaxf(gd * (1.f / 640.f) - mean * mean, 0.f);
      const float rstd = rsqrtf(var + 1e-5f), sh = -mean * rstd;
#pragma unroll
      for (int s = 0; s < 20; ++s)
#pragma unroll
        for (int i = 0; i < 4; ++i) { unsigned u = xf[s][i]; xf[s][i] = pack2(fmaf(bflo(u), rstd, sh), fmaf(bfhi(u), rstd, sh)); }
    }
    f4 acc[5];
#pragma unroll
    for (int c = 0; c < 5; ++c) acc[c] = f4{0, 0, 0, 0};
    const int posl = 16 * (g4 >> 1) + 4 * (g4 & 1) + q4;
    LAS char* kvl = lds + posl * 160 + p4 * 8;
    auto kv_steps = [&](int s0, int s1) {
#pragma unroll
      for (int s = s0; s < s1; ++s) {
        LAS char* b = kvl + (s < 16 ? WREG + s * 5120 : XREG + (s - 16) * 5120);
#pragma unroll
        for (int c = 0; c < 5; ++c) acc[c] = mfma16(xf[s], trfrag(b + c * 32, 8 * 160), acc[c]);
      }
    };
    kv_steps(0, 8);
    if (wave < 4) WAITV(11); else WAITV(9);
    BAR();
    kv_steps(8, 16);
    if (wave < 4) WAITV(6);
    BAR();
    kv_steps(16, 20);
    BAR();
    {
      constexpr unsigned EX = 81920;
      LAS char* exw = lds + EX + wave * 3072 + l * 8;
      auto pk4 = [](float a, float b, float c, float d) { return v2u{pack2(a, b), pack2(c, d)}; };
#pragma unroll
      for (int c = 0; c < 3; ++c) *(LAS v2u*)(exw + c * 512) = pk4(phi(acc[c][0]), phi(acc[c][1]), phi(acc[c][2]), phi(acc[c][3]));
      v2u v2 = pk4(acc[2][0], acc[2][1], acc[2][2], acc[2][3]);
      if (r16 < 2) v2 = v2u{0x3f803f80u, 0x3f803f80u}; else if (r16 < 8) v2 = v2u{0u, 0u};
      *(LAS v2u*)(exw + 3 * 512) = v2;
      *(LAS v2u*)(exw + 4 * 512) = pk4(acc[3][0], acc[3][1], acc[3][2], acc[3][3]);
      *(LAS v2u*)(exw + 5 * 512) = pk4(acc[4][0], acc[4][1], acc[4][2], acc[4][3]);
      BAR();
      const int ta = wave < 6 ? wave % 3 : wave - 5, tb = wave < 6 ? wave / 3 : 2;
      LAS char* ea = lds + EX + ta * 512 + l * 8;
      LAS char* eb = lds + EX + (3 + tb) * 512 + l * 8;
      f4 kv = {0, 0, 0, 0};
#pragma unroll
      for (int w = 0; w < 8; ++w) kv = mfma16k(*(LAS v2u*)(ea + w * 3072), *(LAS v2u*)(eb + w * 3072), kv);
      st16_sc1(__builtin_bit_cast(v4u, kv), rws, l * 16, WS_KVP + ((T * 16 + hp) * 8 + wave) * 1024);
      WAITV(0);
      BAR();
      if constexpr (FU) { if (tid == 0) set_flag(ws, WS_F1 + (T * 16 + hp) * 128, tag); }
    }
    f4 aq[3];
#pragma unroll
    for (int c = 0; c < 3; ++c) aq[c] = f4{0, 0, 0, 0};
    LAS char* ql = lds + QIMG + posl * 96 + p4 * 8;
#pragma unroll
    for (int s = 0; s < 20; ++s)
#pragma unroll
      for (int c = 0; c < 3; ++c) aq[c] = mfma16(trfrag(ql + s * 3072 + c * 32, 8 * 96), xf[s], aq[c]);
#pragma unroll
    for (int c = 0; c < 3; ++c) pq[c] = v2u{pack2(phi(aq[c][0]), phi(aq[c][1])), pack2(phi(aq[c][2]), phi(aq[c][3]))};
    if (g4 >= 2) pq[2] = v2u{0u, 0u};
    if constexpr (!(PH & 2)) {
#pragma unroll
      for (int c = 0; c < 3; ++c) *(GAS v2u*)(ws + WS_PQ + ((bid * 8 + wave) * 3 + c) * 512 + l * 8) = pq[c];
    }
  }

  if constexpr (PH & 2) {
    if constexpr (!(PH & 1)) {
#pragma unroll
      for (int c = 0; c < 3; ++c) pq[c] = *(GAS v2u*)(ws + WS_PQ + ((bid * 8 + wave) * 3 + c) * 512 + l * 8);
    }
    if constexpr (FU) {
      if (wave == 7) poll_flags(ws, l < 16 ? WS_F1 + (l * 16 + hp) * 128 : 0xffffffffu, tag);
      BAR();
    }
    constexpr unsigned AF = 0;
    {
      v4u part[16];
#pragma unroll
      for (int t = 0; t < 16; ++t) LD_SC1(part[t], l * 16, rws, WS_KVP + ((t * 16 + hp) * 8 + wave) * 1024);
      f4 sum = {0, 0, 0, 0};
      asm volatile("s_waitcnt vmcnt(0)" : "+v"(part[0]), "+v"(part[1]), "+v"(part[2]), "+v"(part[3]), "+v"(part[4]), "+v"(part[5]), "+v"(part[6]), "+v"(part[7]),
                   "+v"(part[8]), "+v"(part[9]), "+v"(part[10]), "+v"(part[11]), "+v"(part[12]), "+v"(part[13]), "+v"(part[14]), "+v"(part[15])::"memory");
#pragma unroll
      for (int t = 0; t < 16; ++t) sum += __builtin_bit_cast(f4, part[t]);
      const int ta = wave < 6 ? wave % 3 : wave - 5, tb = wave < 6 ? wave / 3 : 2;
      const int d0 = 16 * ta + 4 * g4;
      const int hd = d0 < 20 ? 0 : d0 < 40 ? 1 : 2;
      int he;
      if (tb == 0) he = r16 == 0 ? 0 : r16 == 1 ? 1 : r16 < 8 ? 3 : 0;
      else if (tb == 1) he = (8 + r16) < 20 ? 0 : 1;
      else he = 1;
      v2u a = v2u{pack2(sum[0], sum[1]), pack2(sum[2], sum[3])};
      if (hd != he) a = v2u{0u, 0u};
      *(LAS v2u*)(lds + AF + wave * 512 + l * 8) = a;
    }
    BAR();
    v2u af[8];
#pragma unroll
    for (int j = 0; j < 8; ++j) af[j] = *(LAS v2u*)(lds + AF + j * 512 + l * 8);
    const f4 z4 = {0, 0, 0, 0};
    f4 e2 = mfma16k(af[0], pq[0], z4); e2 = mfma16k(af[1], pq[1], e2); e2 = mfma16k(af[2], pq[2], e2);
    f4 e3 = mfma16k(af[3], pq[0], z4); e3 = mfma16k(af[4], pq[1], e3); e3 = mfma16k(af[5], pq[2], e3);
    f4 e4 = mfma16k(af[6], pq[1], z4); e4 = mfma16k(af[7], pq[2], e4);
    const float denA = bfr(bfr(__shfl(e2[0], r16)) + 1e-6f), denB = bfr(bfr(__shfl(e2[1], r16)) + 1e-6f);
    const float rA = 1.f / denA, rB = 1.f / denB;
    auto outq = [&](f4 e, float rr, int quad) {
      v2u o = v2u{pack2(bfr(e[0]) * rr, bfr(e[1]) * rr), pack2(bfr(e[2]) * rr, bfr(e[3]) * rr)};
      const int kq = 10 * hp + quad;
      const unsigned off = (((4 * T + (wave >> 1)) * 40 + (kq >> 2)) * 64 + (kq & 1) * 32 + 16 * (wave & 1) + r16) * 16 + ((kq >> 1) & 1) * 8;
      st8_sc1(o, rws, off, WS_OFR);
    };
    if (g4 >= 2) outq(e2, rA, g4 - 2);
    outq(e3, g4 < 3 ? rA : rB, 2 + g4);
    outq(e4, rB, 6 + g4);
    WAITV(0);
    BAR();
    if constexpr (FU) { if (tid == 0) set_flag(ws, WS_F2 + (T * 16 + hp) * 128, tag); }
  }

  if constexpr (PH & 4) {
    constexpr unsigned WOB = 81920;
    const rsrc_t rwo = mkrs(p.wo, DM * DM * 2);
    if (wact) {
      const unsigned v = img128_voff(l, 1280);
#pragma unroll
      for (int i = 0; i < 10; ++i) glds(ldsb + WOB + (10 * wave + i) * 1024, v, rwo, (10 * wave + i) * 8 * 1280 + wcu * 128);
    }
    const int tgl = wave % 3, kh = wave / 3, tg = 3 * wt + tgl;
    const bool cw = wact && wave < 6 && tg < 64;
    const int hh = l >> 5, r32 = l & 31;
    v2u xr[8];
    if (cw && kh == 0) {
      const char* xp = (const char*)p.x + (size_t)(32 * tg + r32) * 1280 + wcu * 128 + hh * 8;
#pragma unroll
      for (int i = 0; i < 8; ++i) xr[i] = *(const GAS v2u*)(xp + (i >> 2) * 64 + (i & 3) * 16);
    }
    if constexpr (FU) {
      if (wave == 7 && wact) {
        const int t0 = (96 * wt) >> 7, t1 = min(96 * wt + 95, SEQ - 1) >> 7;
        const int tt = t0 + (l >> 4);
        poll_flags(ws, (tt <= t1) ? WS_F2 + (tt * 16 + (l & 15)) * 128 : 0xffffffffu, tag);
      }
    }
    WAITV(0);
    BAR();
    f16v acc[2];
#pragma unroll
    for (int c = 0; c < 2; ++c)
#pragma unroll
      for (int i = 0; i < 16; ++i) acc[c][i] = 0.f;
    if (cw) {
      v4u of[20];
#pragma unroll
      for (int s = 0; s < 20; ++s) LD_SC1(of[s], l * 16, rws, WS_OFR + (tg * 40 + 20 * kh + s) * 1024);
      LAS char* w0 = lds + WOB + img128_lane(l, 0) + kh * 20 * 2048;
      LAS char* w1 = lds + WOB + img128_lane(l, 1) + kh * 20 * 2048;
sfor<0, 20>([&](auto S) {
        constexpr int s = S;
        waitv_on<19 - s>(of[s]);
        acc[0] = mfma32(trfrag(w0 + s * 2048, 1024), of[s], acc[0]);
        acc[1] = mfma32(trfrag(w1 + s * 2048, 1024), of[s], acc[1]);
      });
    }
    BAR();
    constexpr unsigned RED = 81920;
    if (cw && kh == 1) {
#pragma unroll
      for (int c = 0; c < 2; ++c)
#pragma unroll
        for (int i = 0; i < 4; ++i)
          *(LAS f4*)(lds + RED + tgl * 8192 + (c * 4 + i) * 1024 + l * 16) = f4{acc[c][4 * i], acc[c][4 * i + 1], acc[c][4 * i + 2], acc[c][4 * i + 3]};
    }
    BAR();
    if (cw && kh == 0) {
      float s1 = 0.f, s2 = 0.f;
#pragma unroll
      for (int c = 0; c < 2; ++c) {
        unsigned hp2[8];
#pragma unroll
        for (int i = 0; i < 4; ++i) {
          const f4 o = *(LAS f4*)(lds + RED + tgl * 8192 + (c * 4 + i) * 1024 + l * 16);
          const v2u xx = xr[c * 4 + i];
          const float h0 = bfr(bflo(xx.x) + bfr(acc[c][4 * i] + o[0])), h1 = bfr(bfhi(xx.x) + bfr(acc[c][4 * i + 1] + o[1]));
          const float h2 = bfr(bflo(xx.y) + bfr(acc[c][4 * i + 2] + o[2])), h3 = bfr(bfhi(xx.y) + bfr(acc[c][4 * i + 3] + o[3]));
          s1 += (h0 + h1) + (h2 + h3);
          s2 += (h0 * h0 + h1 * h1) + (h2 * h2 + h3 * h3);
          hp2[2 * i] = pack2(h0, h1); hp2[2 * i + 1] = pack2(h2, h3);
        }
#pragma unroll
        for (int s = 0; s < 2; ++s) {
          hkeep[2 * c + s] = v4u{hp2[4 * s], hp2[4 * s + 1], hp2[4 * s + 2], hp2[4 * s + 3]};
          st16_sc1(hkeep[2 * c + s], rws, l * 16, WS_HFR + (tg * 40 + 4 * wcu + 2 * c + s) * 1024);
        }
      }
      s1 += __shfl_xor(s1, 32); s2 += __shfl_xor(s2, 32);
      if (hh == 0) st8_sc1(v2u{__builtin_bit_cast(unsigned, s1), __builtin_bit_cast(unsigned, s2)}, rws, (32 * tg + r32) * 8, WS_HST + wcu * 2048 * 8);
    }
    WAITV(0);
    BAR();
    if constexpr (FU) { if (tid == 0 && wact) set_flag(ws, WS_F3 + wtask * 128, tag); }
    if constexpr (FU) { if (tid == 0 && bid == 0) __hip_atomic_store((gu64*)(ws + WS_EPOCH), tag, __ATOMIC_RELAXED, __HIP_MEMORY_SCOPE_AGENT); }
  }

  if constexpr (PH & 8) {
    const rsrc_t rg = mkrs(p.wg, DM * FF * 2), ru = mkrs(p.wu, DM * FF * 2);
    if (gact) {
      const unsigned v = img128_voff(l, 3456);
#pragma unroll
      for (int i = 0; i < 5; ++i) {
        const int ks = 5 * wave + i;
        glds(ldsb + ks * 4096, v, rg, ks * 16 * 3456 + gu * 128);
        glds(ldsb + ks * 4096 + 1024, v, rg, (ks * 16 + 8) * 3456 + gu * 128);
        glds(ldsb + ks * 4096 + 2048, v, ru, ks * 16 * 3456 + gu * 128);
        glds(ldsb + ks * 4096 + 3072, v, ru, (ks * 16 + 8) * 3456 + gu * 128);
      }
    }
    const int tg = 8 * gtt + wave, r32 = l & 31;
    if constexpr (FU) {
      if (wave == 7 && gact) {
        const int t0 = (8 * gtt) / 3, t1 = (8 * gtt + 7) / 3;
        const int tt = t0 + l / 10;
        poll_flags(ws, (tt <= t1 && l < 40) ? WS_F3 + (tt * 10 + l % 10) * 128 : 0xffffffffu, tag);
      }
    }
    WAITV(0);
    BAR();
    if (gact) {
      f2 st[10];
#pragma unroll
      for (int c = 0; c < 10; ++c) ld8_sc1(st[c], (32 * tg + r32) * 8, rws, WS_HST + c * 2048 * 8);
      v4u hf[40];
#pragma unroll
      for (int s = 0; s < 40; ++s) LD_SC1(hf[s], l * 16, rws, WS_HFR + (tg * 40 + s) * 1024);
      asm volatile("s_waitcnt vmcnt(40)" : "+v"(st[0]), "+v"(st[1]), "+v"(st[2]), "+v"(st[3]), "+v"(st[4]), "+v"(st[5]), "+v"(st[6]), "+v"(st[7]), "+v"(st[8]), "+v"(st[9])::"memory");
      float s1 = 0.f, s2 = 0.f;
#pragma unroll
      for (int c = 0; c < 10; ++c) { s1 += st[c][0]; s2 += st[c][1]; }
      const float mean = s1 * (1.f / 640.f);
      const float var = fmaxf(s2 * (1.f / 640.f) - mean * mean, 0.f);
      const float rstd = rsqrtf(var + 1e-5f), sh = -mean * rstd;
      f16v acc[4];
#pragma unroll
      for (int c = 0; c < 4; ++c)
#pragma unroll
        for (int i = 0; i < 16; ++i) acc[c][i] = 0.f;
      LAS char* w0 = lds + img128_lane(l, 0);
      LAS char* w1 = lds + img128_lane(l, 1);
sfor<0, 40>([&](auto S) {
        constexpr int s = S;
        waitv_on<39 - s>(hf[s]);
        v4u b;
#pragma unroll
        for (int i = 0; i < 4; ++i) { unsigned u = hf[s][i]; b[i] = pack2(fmaf(bflo(u), rstd, sh), fmaf(bfhi(u), rstd, sh)); }
        acc[0] = mfma32(trfrag(w0 + s * 4096, 1024), b, acc[0]);
        acc[1] = mfma32(trfrag(w1 + s * 4096, 1024), b, acc[1]);
        acc[2] = mfma32(trfrag(w0 + s * 4096 + 2048, 1024), b, acc[2]);
        acc[3] = mfma32(trfrag(w1 + s * 4096 + 2048, 1024), b, acc[3]);
      });
#pragma unroll
      for (int c = 0; c < 2; ++c) {
        unsigned ap[8];
#pragma unroll
        for (int i = 0; i < 8; ++i) {
          float a[2];
#pragma unroll
          for (int j = 0; j < 2; ++j) {
            const float gg = bfr(acc[c][2 * i + j]), uu = bfr(acc[2 + c][2 * i + j]);
            const float y = 0.7978845608028654f * (gg + 0.044715f * gg * gg * gg);
            const float ge = bfr(gg / (1.f + __expf(-2.f * y)));
            a[j] = ge * uu;
          }
          ap[i] = pack2(a[0], a[1]);
        }
#pragma unroll
        for (int s = 0; s < 2; ++s)
          st16_sc1(v4u{ap[4 * s], ap[4 * s + 1], ap[4 * s + 2], ap[4 * s + 3]}, rws, l * 16, WS_AFR + (tg * 108 + 4 * gu + 2 * c + s) * 1024);
      }
    }
    WAITV(0);
    BAR();
    if constexpr (FU) { if (tid == 0 && gact) set_flag(ws, WS_F4 + (gtt * 27 + gu) * 128, tag); }
  }

  if constexpr (PH & 16) {
    const rsrc_t rd = mkrs(p.wd, FF * DM * 2);
    const int tgl = wave % 3, kh = wave / 3, tg = 3 * wt + tgl;
    const bool cw = wact && wave < 6 && tg < 64;
    const int hh = l >> 5, r32 = l & 31;
    const unsigned vw = img128_voff(l, 1280);
    auto issue_w = [&](int k2, int j, int slotj) {
      glds(ldsb + slotj * 2048, vw, rd, (54 * k2 + j) * 16 * 1280 + wcu * 128);
      glds(ldsb + slotj * 2048 + 1024, vw, rd, ((54 * k2 + j) * 16 + 8) * 1280 + wcu * 128);
    };
    if (wact) {
#pragma unroll
      for (int i = 0; i < 10; ++i) {
        const int n = 10 * wave + i, k2 = n / 40, j = n % 40;
        issue_w(k2, j, j < 20 ? 20 * k2 + j : 40 + 20 * k2 + (j - 20));
      }
    }
    if constexpr (!(PH & 4)) {
      if (cw && kh == 0) {
#pragma unroll
        for (int i = 0; i < 4; ++i) hkeep[i] = *(const GAS v4u*)(ws + WS_HFR + (tg * 40 + 4 * wcu + i) * 1024 + l * 16);
      }
    }
    if constexpr (FU) {
      if (wave == 7 && wact) {
        const int t0 = (3 * wt) >> 3, t1 = min(3 * wt + 2, 63) >> 3;
        const int tt = t0 + l / 27;
        poll_flags(ws, (tt <= t1 && l < 54) ? WS_F4 + (tt * 27 + l % 27) * 128 : 0xffffffffu, tag);
      }
    }
    WAITV(0);
    BAR();
    f16v acc[2];
#pragma unroll
    for (int c = 0; c < 2; ++c)
#pragma unroll
      for (int i = 0; i < 16; ++i) acc[c][i] = 0.f;
    constexpr int WIN = 24;
    v4u af[WIN];
    const unsigned abase = WS_AFR + (tg * 108 + 54 * kh) * 1024;
    LAS char* w0 = lds + img128_lane(l, 0) + kh * 20 * 2048;
    LAS char* w1 = lds + img128_lane(l, 1) + kh * 20 * 2048;
    auto step = [&](auto S) {
      constexpr int s = S;
      constexpr int rem = 53 - s;
      waitv_on<(rem < WIN - 1 ? rem : WIN - 1)>(af[s % WIN]);
      constexpr int so = (s < 20 ? s : s < 40 ? 40 + (s - 20) : s - 40) * 2048;
      const v4u b = af[s % WIN];
      acc[0] = mfma32(trfrag(w0 + so, 1024), b, acc[0]);
      acc[1] = mfma32(trfrag(w1 + so, 1024), b, acc[1]);
      if constexpr (s + WIN < 54) LD_SC1(af[s % WIN], l * 16, rws, abase + (s + WIN) * 1024);
    };
    if (cw) {
#pragma unroll
      for (int s = 0; s < WIN; ++s) LD_SC1(af[s], l * 16, rws, abase + s * 1024);
      sfor<0, 20>(step);
    }
    BAR();
    if (wact && wave >= 6) {
#pragma unroll
      for (int j = 40; j < 54; ++j) issue_w(wave - 6, j, 20 * (wave - 6) + (j - 40));
      WAITV(0);
    }
    if (cw) sfor<20, 40>(step);
    BAR();
    if (cw) sfor<40, 54>(step);
    BAR();
    constexpr unsigned RED = 0, OUTT = 32768;
    if (cw && kh == 1) {
#pragma unroll
      for (int c = 0; c < 2; ++c)
#pragma unroll
        for (int i = 0; i < 4; ++i)
          *(LAS f4*)(lds + RED + tgl * 8192 + (c * 4 + i) * 1024 + l * 16) = f4{acc[c][4 * i], acc[c][4 * i + 1], acc[c][4 * i + 2], acc[c][4 * i + 3]};
    }
    BAR();
    if (cw && kh == 0) {
      LAS char* ot = lds + OUTT + tgl * 8192;
#pragma unroll
      for (int c = 0; c < 2; ++c)
#pragma unroll
        for (int i = 0; i < 4; ++i) {
          const f4 o = *(LAS f4*)(lds + RED + tgl * 8192 + (c * 4 + i) * 1024 + l * 16);
          const unsigned h01 = hkeep[2 * c + (i >> 1)][2 * (i & 1)], h23 = hkeep[2 * c + (i >> 1)][2 * (i & 1) + 1];
          const float o0 = bflo(h01) + bfr(acc[c][4 * i] + o[0]), o1 = bfhi(h01) + bfr(acc[c][4 * i + 1] + o[1]);
          const float o2 = bflo(h23) + bfr(acc[c][4 * i + 2] + o[2]), o3 = bfhi(h23) + bfr(acc[c][4 * i + 3] + o[3]);
          *(LAS v2u*)(ot + r32 * 144 + c * 64 + i * 16 + hh * 8) = v2u{pack2(o0, o1), pack2(o2, o3)};
        }
      WAITL0();
      char* op = (char*)p.out + (size_t)(32 * tg) * 1280 + wcu * 128;
#pragma unroll
      for (int i = 0; i < 4; ++i) {
        const int row = 8 * i + (l >> 3);
        const v4u v = *(LAS v4u*)(ot + row * 144 + (l & 7) * 16);
        *(GAS v4u*)(op + row * 1280 + (l & 7) * 16) = v;
      }
    }
  }
}

extern "C" void kernel_launch(void* const* d_in, const int* in_sizes, int n_in, void* d_out, int out_size, void* d_ws, size_t ws_size,
                              hipStream_t stream) {
  Params p;
  p.x = d_in[0]; p.wqkv = d_in[1]; p.wo = d_in[2]; p.wg = d_in[4]; p.wu = d_in[5]; p.wd = d_in[6]; p.out = d_out; p.ws = (char*)d_ws;
  (void)hipFuncSetAttribute((const void*)blk_kernel<31>, hipFuncAttributeMaxDynamicSharedMemorySize, LDS_BYTES);
  blk_kernel<31><<<256, 512, LDS_BYTES, stream>>>(p);
}
```

```cpp
#include <hip/hip_runtime.h>
#include <stdint.h>
#include <type_traits>

#define FUSED 1

#define SL1 12
#define SL2 16
#define SL3 8
#define GAS __attribute__((address_space(1)))
#define LAS __attribute__((address_space(3)))
#define DI __device__ __forceinline__
typedef unsigned v4u __attribute__((ext_vector_type(4)));
typedef unsigned v2u __attribute__((ext_vector_type(2)));
typedef float f4 __attribute__((ext_vector_type(4)));
typedef float f2 __attribute__((ext_vector_type(2)));
typedef float f16v __attribute__((ext_vector_type(16)));
typedef short s4v __attribute__((ext_vector_type(4)));
typedef __bf16 b8v __attribute__((ext_vector_type(8)));
typedef __bf16 b2v __attribute__((ext_vector_type(2)));
typedef __amdgpu_buffer_rsrc_t rsrc_t;
typedef GAS unsigned long long gu64;

constexpr int SEQ = 2048, DM = 640, FF = 1728;
constexpr unsigned WS_EPOCH = 0, WS_F1 = 4096;
constexpr unsigned WS_ST1 = 196608;
constexpr unsigned WS_F0 = 262144;
constexpr unsigned WS_KVP = WS_F0 + 131072;
constexpr unsigned WS_OFR = WS_KVP + 16 * 16 * 8 * 1024;
constexpr unsigned WS_HFR = WS_OFR + 64 * 40 * 1024;
constexpr unsigned WS_HST = WS_HFR + 64 * 40 * 1024;
constexpr unsigned WS_AFR = WS_HST + 10 * 2048 * 8;
#define AFR_TG (108 * 1024)
constexpr unsigned WS_PKW = WS_AFR + 64 * AFR_TG;
constexpr unsigned WS_F2 = WS_PKW + 16 * 153600;
constexpr unsigned WS_F3 = WS_F2 + 1024 * 128;
constexpr unsigned WS_F4 = WS_F3 + 640 * 128;
constexpr unsigned WS_END = WS_F4 + 1728 * 128;
constexpr unsigned LDS_BYTES = 163840, SCR = 157696;

struct Params {
  const void* x; const void* wqkv; const void* wo; const void* wg; const void* wu; const void* wd; void* out; char* ws;
};

DI f4 mfma16(v4u a, v4u b, f4 c) { return __builtin_amdgcn_mfma_f32_16x16x32_bf16(__builtin_bit_cast(b8v, a), __builtin_bit_cast(b8v, b), c, 0, 0, 0); }
DI f4 mfma16k(v2u a, v2u b, f4 c) { return __builtin_amdgcn_mfma_f32_16x16x16bf16_1k(__builtin_bit_cast(s4v, a), __builtin_bit_cast(s4v, b), c, 0, 0, 0); }
DI f16v mfma32(v4u a, v4u b, f16v c) { return __builtin_amdgcn_mfma_f32_32x32x16_bf16(__builtin_bit_cast(b8v, a), __builtin_bit_cast(b8v, b), c, 0, 0, 0); }
DI unsigned pack2(float lo, float hi) { b2v v = {(__bf16)lo, (__bf16)hi}; return __builtin_bit_cast(unsigned, v); }
DI float u2f(unsigned u) { return __builtin_bit_cast(float, u); }
DI unsigned f2u(float f) { return __builtin_bit_cast(unsigned, f); }
DI float bfr(float x) { return (float)(__bf16)x; }
DI float bflo(unsigned u) { return __builtin_bit_cast(float, u << 16); }
DI float bfhi(unsigned u) { return __builtin_bit_cast(float, u & 0xffff0000u); }
DI rsrc_t mkrs(const void* p, unsigned bytes) { return __builtin_amdgcn_make_buffer_rsrc((void*)p, 0, bytes, 0x00020000); }
DI unsigned opq(unsigned v) { asm volatile("" : "+v"(v)); return v; }
DI v2u trread(unsigned a) { return __builtin_bit_cast(v2u, __builtin_amdgcn_ds_read_tr16_b64_v4i16((LAS s4v*)(size_t)a)); }
DI v4u trfrag(unsigned a, int toff) { v2u x = trread(a), y = trread(a + toff); return v4u{x.x, x.y, y.x, y.y}; }
template <class V> DI V ldsld(unsigned a) { return *(LAS V*)(size_t)a; }
template <class V> DI void ldsst(unsigned a, V v) { *(LAS V*)(size_t)a = v; }
DI void glds(unsigned m0v, unsigned voff, rsrc_t rs, unsigned soff) {
  asm volatile("s_mov_b32 m0, %0\n\ts_nop 0\n\tbuffer_load_dwordx4 %1, %2, %3 offen lds" ::"s"(m0v), "v"(voff), "s"(rs), "s"(soff) : "m0", "memory");
}
DI void glds_sc1(unsigned m0v, unsigned voff, rsrc_t rs, unsigned soff) {
  asm volatile("s_mov_b32 m0, %0\n\ts_nop 0\n\tbuffer_load_dwordx4 %1, %2, %3 offen sc1 lds" ::"s"(m0v), "v"(voff), "s"(rs), "s"(soff) : "m0", "memory");
}
DI void ld_sc1(v4u& dst, unsigned voff, rsrc_t rs, unsigned soff) {
  asm volatile("buffer_load_dwordx4 %0, %1, %2, %3 offen sc1" : "=v"(dst) : "v"(voff), "s"(rs), "s"(soff));
}
DI void ld8_sc1(f2& dst, unsigned voff, rsrc_t rs, unsigned soff) {
  asm volatile("buffer_load_dwordx2 %0, %1, %2, %3 offen sc1" : "=v"(dst) : "v"(voff), "s"(rs), "s"(soff));
}
DI void warm(unsigned& junk, unsigned voff, rsrc_t rs, unsigned soff) {
  asm volatile("buffer_load_dword %0, %1, %2, %3 offen" : "+v"(junk) : "v"(voff), "s"(rs), "s"(soff));
}
#define WAITV(n) asm volatile("s_waitcnt vmcnt(%0)" ::"n"(n) : "memory")
template <int N> DI void waitv_on(v4u& r) { asm volatile("s_waitcnt vmcnt(%1)" : "+v"(r) : "n"(N)); }
template <int I, int E, class Fn> DI void sfor(Fn&& f) {
  if constexpr (I < E) { f(std::integral_constant<int, I>{}); sfor<I + 1, E>(f); }
}
#define WAITL0() asm volatile("s_waitcnt lgkmcnt(0)" ::: "memory")
#define BAR() asm volatile("s_waitcnt lgkmcnt(0)\n\ts_barrier" ::: "memory")
#define SCHED() __builtin_amdgcn_sched_barrier(0)
constexpr int AUX_SC1 = 16;
DI void st16_sc1(v4u v, rsrc_t rs, unsigned voff, unsigned soff) { __builtin_amdgcn_raw_buffer_store_b128(v, rs, voff, soff, AUX_SC1); }
DI void st8_sc1(v2u v, rsrc_t rs, unsigned voff, unsigned soff) { __builtin_amdgcn_raw_buffer_store_b64(v, rs, voff, soff, AUX_SC1); }

DI f2 phi2(f2 t) {
  const f2 a = t * 1.4426950408889634f, b = t + 1e-6f;
  return f2{fmaxf(b[0], 1e-6f), fmaxf(b[1], 1e-6f)} + f2{__builtin_amdgcn_exp2f(fminf(a[0], 0.f)), __builtin_amdgcn_exp2f(fminf(a[1], 0.f))};
}
DI unsigned packf2(f2 v) { return pack2(v[0], v[1]); }

#define STAMP(i) do {} while (0)
#define CSTAMP(i) do {} while (0)
constexpr unsigned WS_STAMP = 64u << 20;

DI void set_flag(char* ws, unsigned off, unsigned long long tag) {
  __hip_atomic_store((gu64*)(ws + off), tag, __ATOMIC_RELAXED, __HIP_MEMORY_SCOPE_AGENT);
}
constexpr unsigned NOFLAG = 0x80000000u;
struct Polls { v2u v[3]; };
DI void poll_issue(v2u& d, rsrc_t rs, unsigned off) { asm volatile("buffer_load_dwordx2 %0, %1, %2, 0 offen sc1" : "=v"(d) : "v"(off), "s"(rs)); }
DI void poll_flags(Polls& pv, rsrc_t rs, unsigned off, unsigned long long tag) {
  const unsigned tlo = (unsigned)tag, thi = (unsigned)(tag >> 32);
  const bool none = off == NOFLAG;
  poll_issue(pv.v[0], rs, off); __builtin_amdgcn_s_sleep(2);
  poll_issue(pv.v[1], rs, off); __builtin_amdgcn_s_sleep(2);
  poll_issue(pv.v[2], rs, off);
  for (unsigned spins = 0; spins < (1u << 15); ++spins) {
    asm volatile("s_waitcnt vmcnt(2)" : "+v"(pv.v[0]));
    if (__all(none || (pv.v[0].x == tlo && pv.v[0].y == thi))) break;
    poll_issue(pv.v[0], rs, off);
    asm volatile("s_waitcnt vmcnt(2)" : "+v"(pv.v[1]));
    if (__all(none || (pv.v[1].x == tlo && pv.v[1].y == thi))) break;
    poll_issue(pv.v[1], rs, off);
    asm volatile("s_waitcnt vmcnt(2)" : "+v"(pv.v[2]));
    if (__all(none || (pv.v[2].x == tlo && pv.v[2].y == thi))) break;
    poll_issue(pv.v[2], rs, off);
  }
  asm volatile("" ::: "memory");
}
DI void keep_polls(Polls& pv) { asm volatile("" : "+v"(pv.v[0]), "+v"(pv.v[1]), "+v"(pv.v[2])); }

DI unsigned img128_voff(int l, int rowstride) { int r = l >> 3, c = (l & 7) ^ (((r >> 1) & 1) << 2); return r * rowstride + c * 16; }
DI unsigned img128_lane(int l, int ct) {
  int hh = l >> 5, blk = (l >> 4) & 1, q = (l & 15) >> 2, p = l & 3;
  return (4 * hh + q) * 128 + 64 * (ct ^ (q >> 1)) + 32 * blk + 8 * p;
}

DI unsigned kv_index(int j, int l) {
  const int c = l & 31, hh = l >> 5;
  const int ra = c == 0 ? 0 : c - 7, rb = c == 1 ? 0 : c - 27;
  const bool va = c == 0 || (c >= 8 && c < 28), vb = c == 1 || c >= 28, v2 = c < 16;
  bool ok; int r;
  if (j < 2) { ok = va; r = 42 * j + 21 * hh + ra; }
  else if (j == 2) { ok = hh ? vb : va; r = 84 + (hh ? 21 + rb : ra); }
  else if (j == 3) { ok = vb; r = 110 + 5 * hh + rb; }
  else if (j == 4) { ok = hh && v2; r = 120 + c; }
  else if (j == 5) { ok = v2; r = 136 + 16 * hh + c; }
  else if (j == 6) { ok = vb; r = 168 + 5 * hh + rb; }
  else { ok = v2; r = 178 + 16 * hh + c; }
  return ok ? r : 210;
}

template <int PH>
__global__ void __launch_bounds__(512, 2) blk_kernel(Params p) {
  extern __shared__ __attribute__((aligned(16))) char smem[];
  const unsigned lds = (unsigned)(size_t)(LAS char*)smem;
  const int tid = threadIdx.x, l = tid & 63;
  const int wave = __builtin_amdgcn_readfirstlane(tid >> 6);
  int wv2 = wave >> 1, wv1 = wave & 1;
  asm volatile("" : "+s"(wv2), "+s"(wv1));
  const int bid = blockIdx.x, x8 = bid & 7, slot = bid >> 3;
  char* ws = p.ws;
  const rsrc_t rws = mkrs(ws, WS_END);
  constexpr bool FU = true;
  static_assert(PH == 31, "one launch");
  STAMP(0); CSTAMP(30);
  unsigned long long tag = *(const __attribute__((address_space(4))) unsigned long long*)(size_t)(ws + WS_EPOCH);
  auto take_tag = [&]() { asm volatile("" : "+s"(tag)); tag += 1ull; };

  const int T = 4 * (x8 >> 1) + (slot & 3), hp = 8 * (x8 & 1) + (slot >> 2);
  const int wstart = (x8 * 55 + 1) >> 1, wcnt = 28 - (x8 & 1);
  const bool wact = slot < wcnt;
  const int wtask = wact ? wstart + slot : 219, wt = wtask / 10, wcu = wtask % 10;
  const int gtt = 2 * (x8 >> 1) + (slot & 1);
  const bool gact = (slot >> 1) < ((x8 & 1) ? 13 : 14);
  const int gu = gact ? 14 * (x8 & 1) + (slot >> 1) : 0;

  const int g4 = l >> 4, r16 = l & 15, q4 = r16 >> 2, p4 = l & 3;
  const int hh = l >> 5, r32 = l & 31;
  v4u pq[3];
  unsigned srcj[8];
  v4u hkeep[4];
  unsigned junk = 0;

  auto pf_wo = [&]() {
    const rsrc_t rwo = mkrs(p.wo, DM * DM * 2);
    const unsigned v = img128_voff(l, 1280);
#pragma unroll
    for (int i = 0; i < 40; ++i) { const int n = 40 * (wave - 6) + i; glds(lds + n * 1024, v, rwo, n * 8 * 1280 + wcu * 128); }
  };
  auto pf_gu_half2 = [&](int half) {
    const rsrc_t rg = mkrs(p.wg, DM * FF * 2), ru = mkrs(p.wu, DM * FF * 2);
    const unsigned v = img128_voff(l, 3456);
#pragma unroll
    for (int i = 0; i < 40; ++i) {
      const int ks = 20 * half + 10 * (wave - 6) + (i >> 2), pc = i & 3;
      glds(lds + ks * 4096 + pc * 1024, v, (pc & 2) ? ru : rg, (ks * 16 + (pc & 1) * 8) * 3456 + gu * 128);
    }
  };

  if constexpr (PH & 1) {
    const rsrc_t rx = mkrs(p.x, SEQ * DM * 2);
    constexpr unsigned QIMG = 102400, EX = 0;
    constexpr unsigned GS = 153600;
    constexpr unsigned CNT = SCR, STT = SCR + 64, CSK = SCR + 1152, CSQ = SCR + 2688;
    const v4u ones = {0x3f803f80u, 0x3f803f80u, 0x3f803f80u, 0x3f803f80u};
    if (wave >= 4) {
      const int hw = wave - 4;
      const unsigned vxe = (l >> 3) * 1280 + (((l & 7) ^ (l >> 4)) * 16), vxo = (l >> 3) * 1280 + (((l & 7) ^ (4 + (l >> 4))) * 16);
      STAMP(28);
#pragma unroll
      for (int ci = 0; ci < 10; ++ci) {
        int cc = ci + (hp & 7) + ((hp & 7) >> 2);
        cc = cc >= 10 ? cc - 10 : cc;
#pragma unroll
        for (int rq = 0; rq < 4; ++rq)
          glds(lds + hw * 40960 + cc * 4096 + rq * 1024, (rq & 1) ? vxo : vxe, rx, (128 * T + 32 * hw + 8 * rq) * 1280 + cc * 128);
      }
      STAMP(29);
      take_tag();
      WAITV(0);
      asm volatile("" : "+v"(junk));
      STAMP(1);
      BAR();
      Polls p0;
      poll_flags(p0, rws, l < 16 ? WS_F0 + (hp * 64 + 16 * hw + l) * 128 : NOFLAG, tag);
      BAR();
      __builtin_amdgcn_s_setprio(3);
      if (tid == 256) ldsst<unsigned>(lds + CNT, 0u);
      STAMP(35);
      const unsigned pkw = WS_PKW + hp * 153600;
#pragma unroll
      for (int i = 0; i < 25; ++i) glds_sc1(lds + (4 * i + hw) * 1024, l * 16, rws, pkw + (4 * i + hw) * 1024);
      WAITV(15);
      keep_polls(p0);
      STAMP(36);
      BAR();
      f4 ck[5];
#pragma unroll
      for (int c = 0; c < 5; ++c) ck[c] = f4{0, 0, 0, 0};
      const unsigned kb = lds + l * 16 + hw * 5120;
      auto cs_steps = [&](unsigned a, auto N) {
        v4u cf[N][5];
#pragma unroll
        for (int i = 0; i < N; ++i)
#pragma unroll
          for (int c = 0; c < 5; ++c) cf[i][c] = ldsld<v4u>(a + i * 20480 + c * 1024);
#pragma unroll
        for (int i = 0; i < N; ++i)
#pragma unroll
          for (int c = 0; c < 5; ++c) ck[c] = mfma16(ones, cf[i][c], ck[c]);
      };
      cs_steps(opq(kb), std::integral_constant<int, 2>{});
      STAMP(37);
      WAITV(5);
      STAMP(38);
      BAR();
      cs_steps(opq(kb + 8 * 5120), std::integral_constant<int, 2>{});
      WAITV(0);
      BAR();
#pragma unroll
      for (int i = 0; i < 12; ++i) glds_sc1(lds + (100 + 4 * i + hw) * 1024, l * 16, rws, pkw + (100 + 4 * i + hw) * 1024);
      if (hw < 2) glds_sc1(lds + (148 + hw) * 1024, l * 16, rws, pkw + (148 + hw) * 1024);
      cs_steps(opq(kb + 16 * 5120), std::integral_constant<int, 1>{});
      if (g4 == 0) {
#pragma unroll
        for (int c = 0; c < 5; ++c) ldsst<float>(lds + CSK + (c * 16 + r16) * 16 + hw * 4, ck[c][0]);
      }
      WAITV(0);
      STAMP(40);
      BAR();
      {
        f4 cq[3] = {f4{0, 0, 0, 0}, f4{0, 0, 0, 0}, f4{0, 0, 0, 0}};
        const unsigned qb = opq(lds + QIMG + l * 16 + hw * 2560), qb2 = opq(lds + QIMG + 2048 + (g4 * 8 + (r16 & 7)) * 16 + hw * 2560);
        v4u cf[5][3];
#pragma unroll
        for (int i = 0; i < 5; ++i) { cf[i][0] = ldsld<v4u>(qb + i * 10240); cf[i][1] = ldsld<v4u>(qb + i * 10240 + 1024); cf[i][2] = ldsld<v4u>(qb2 + i * 10240); }
#pragma unroll
        for (int i = 0; i < 5; ++i)
#pragma unroll
          for (int c = 0; c < 3; ++c) cq[c] = mfma16(cf[i][c], ones, cq[c]);
        if (r16 == 0) {
#pragma unroll
          for (int c = 0; c < 3; ++c) ldsst<f4>(lds + CSQ + hw * 192 + (c * 16 + 4 * g4) * 4, cq[c]);
        }
      }
      int ta[2], tb[2];
      unsigned kvo[2];
#pragma unroll
      for (int i = 0; i < 2; ++i) {
        const int j = 2 * hw + i;
        ta[i] = j < 6 ? j % 3 : j - 5; tb[i] = j < 6 ? j / 3 : 2;
        const int d0 = 16 * ta[i] + 4 * g4, h2 = (d0 >> 2) & 1;
        const int col = tb[i] == 1 ? 16 + r16 : r16;
        const int blkj = tb[i] < 2 ? (d0 < 32 ? d0 >> 3 : 6) : (d0 < 32 ? 4 + ((d0 - 16) >> 3) : 7);
        const unsigned idx = d0 < 40 ? kv_index(blkj, col + 32 * h2) : 210u;
        kvo[i] = idx < 210 ? idx * 16 : 0x80000000u;
      }
      STAMP(42);
      BAR();
      STAMP(4);
      f4 d2[2] = {f4{0, 0, 0, 0}, f4{0, 0, 0, 0}};
#pragma unroll
      for (int i = 0; i < 2; ++i) {
        const unsigned ea = lds + EX + ta[i] * 512 + l * 8, eb = lds + EX + (3 + tb[i]) * 512 + l * 8;
#pragma unroll
        for (int w = 0; w < 8; ++w) d2[i] = mfma16k(ldsld<v2u>(ea + w * 3072), ldsld<v2u>(eb + w * 3072), d2[i]);
      }
      STAMP(39);
      const unsigned kvb = WS_KVP + (T * 16 + hp) * 4096;
      st16_sc1(__builtin_bit_cast(v4u, d2[0]), rws, kvo[0], kvb); st16_sc1(__builtin_bit_cast(v4u, d2[1]), rws, kvo[1], kvb);
      WAITV(0);
      if (l == 0) {
        const unsigned old = __hip_atomic_fetch_add((LAS unsigned*)(size_t)(lds + CNT), 1u, __ATOMIC_RELAXED, __HIP_MEMORY_SCOPE_WORKGROUP);
        if (old == 3u) set_flag(ws, WS_F1 + (T * 16 + hp) * 128, tag);
      }
      STAMP(5);
      {
        const int ch = 64 * hw + l;
        const unsigned vo = ch < 210 ? ch * 16 : 0x80000000u;
        Polls p1;
        poll_flags(p1, rws, l < 16 ? WS_F1 + (l * 16 + hp) * 128 : NOFLAG, tag);
        STAMP(7);
        v4u part[16];
#pragma unroll
        for (int t = 0; t < 16; ++t) ld_sc1(part[t], vo, rws, WS_KVP + (t * 16 + hp) * 4096);
        asm volatile("s_waitcnt vmcnt(0)" : "+v"(part[0]), "+v"(part[1]), "+v"(part[2]), "+v"(part[3]), "+v"(part[4]), "+v"(part[5]), "+v"(part[6]), "+v"(part[7]),
                     "+v"(part[8]), "+v"(part[9]), "+v"(part[10]), "+v"(part[11]), "+v"(part[12]), "+v"(part[13]), "+v"(part[14]), "+v"(part[15]));
        keep_polls(p1);
        STAMP(43);
        f4 sum = {0, 0, 0, 0};
#pragma unroll
        for (int t = 0; t < 16; ++t) sum += __builtin_bit_cast(f4, part[t]);
        ldsst<f4>(lds + GS + ch * 16, sum);
      }
      __builtin_amdgcn_s_setprio(0);
    } else {
      const int c4 = wave;
      v4u xf[2][20];
      __builtin_amdgcn_s_setprio(3);
      {
        const rsrc_t rw = mkrs(p.wqkv, DM * 3 * DM * 2);
        v4u wv[3]; unsigned dst[3];
#pragma unroll
        for (int i = 0; i < 3; ++i) {
          const int task = c4 + 4 * i, rg = task >> 1, R0 = 40 * T + 8 * rg;
          const int c = 8 * (task & 1) + (l & 7);
          const bool ok = task < 10 && c < 15;
          const int cb = c < 5 ? 1280 : c >= 10 ? -160 : 2480;
          wv[i] = __builtin_amdgcn_raw_buffer_load_b128(rw, ok ? (R0 + (l >> 3)) * 3840 + 80 * hp + 16 * c + cb : 0x80000000u, 0, 0);
          const int c2 = 8 * (task & 1) + (l >> 3), S = R0 >> 5, g = (R0 >> 3) & 3;
          const int col = 8 * (c2 < 10 ? c2 : c2 - 10) + (l & 7), ct = col >> 4, n = col & 15;
          const unsigned d = c2 < 10 ? S * 5120 + ct * 1024 + (g * 16 + n) * 16 : 102400 + S * 2560 + (ct < 2 ? ct * 1024 + (g * 16 + n) * 16 : 2048 + (g * 8 + n) * 16);
          dst[i] = (task < 10 && c2 < 15) ? d : 0x80000000u;
        }
        const int tok = 128 * T + 8 * hp + 2 * c4 + hh;
        const unsigned xo = tok * 1280 + r32 * 16;
        v4u a0 = __builtin_amdgcn_raw_buffer_load_b128(rx, xo, 0, 0), a1 = __builtin_amdgcn_raw_buffer_load_b128(rx, xo + 512, 0, 0);
        v4u a2 = __builtin_amdgcn_raw_buffer_load_b128(rx, r32 < 16 ? xo + 1024 : 0x80000000u, 0, 0);
        take_tag();
        {
          const unsigned psel = (l & 8) ? 0x03020706u : 0x05040100u;
          const int bp = (((l & 7) << 3) | (l >> 3)) * 4;
#pragma unroll
          for (int i = 0; i < 3; ++i) {
            unsigned d[4];
#pragma unroll
            for (int m = 0; m < 4; ++m) {
              const unsigned x = wv[i][m], y = __builtin_amdgcn_update_dpp(0u, x, 0x128, 0xf, 0xf, false);
              d[m] = __builtin_amdgcn_perm(y, x, psel);
            }
            const auto a = __builtin_amdgcn_permlane16_swap(d[0], d[1], false, false), b = __builtin_amdgcn_permlane16_swap(d[2], d[3], false, false);
            const auto e = __builtin_amdgcn_permlane32_swap(a[0], b[0], false, false), f = __builtin_amdgcn_permlane32_swap(a[1], b[1], false, false);
            const v4u o = {(unsigned)__builtin_amdgcn_ds_bpermute(bp, (int)e[0]), (unsigned)__builtin_amdgcn_ds_bpermute(bp, (int)f[0]),
                           (unsigned)__builtin_amdgcn_ds_bpermute(bp, (int)e[1]), (unsigned)__builtin_amdgcn_ds_bpermute(bp, (int)f[1])};
            st16_sc1(o, rws, dst[i], WS_PKW + hp * 153600);
          }
        }
        float s1 = 0.f, s2 = 0.f;
#pragma unroll
        for (int i = 0; i < 4; ++i) {
          const float v[6] = {bflo(a0[i]), bfhi(a0[i]), bflo(a1[i]), bfhi(a1[i]), bflo(a2[i]), bfhi(a2[i])};
#pragma unroll
          for (int j = 0; j < 6; ++j) { s1 += v[j]; s2 = fmaf(v[j], v[j], s2); }
        }
#pragma unroll
        for (int m = 16; m >= 1; m >>= 1) { s1 += __shfl_xor(s1, m); s2 += __shfl_xor(s2, m); }
        const float mean = s1 * (1.f / 640.f);
        const float rstd = __builtin_amdgcn_rsqf(fmaxf(s2 * (1.f / 640.f) - mean * mean, 0.f) + 1e-5f);
        if (r32 == 0) st16_sc1(v4u{f2u(-mean * rstd), (unsigned)tag, f2u(rstd), (unsigned)tag}, rws, tok * 16, WS_ST1);
        WAITV(0);
        if (l == 0) set_flag(ws, WS_F0 + ((hp * 16 + T) * 4 + c4) * 128, tag);
        STAMP(44);
      }
      __builtin_amdgcn_s_setprio(0);
      const int fx = (r16 >> 1) & 7;
      const unsigned xb0 = opq(lds + c4 * 40960 + r16 * 128 + ((g4 ^ fx) * 16)), xb1 = opq(lds + c4 * 40960 + r16 * 128 + ((g4 ^ fx ^ 4) * 16));
      BAR();
#pragma unroll
      for (int s = 0; s < 20; ++s)
#pragma unroll
        for (int u = 0; u < 2; ++u) xf[u][s] = ldsld<v4u>(((s & 1) ? xb1 : xb0) + (s >> 1) * 4096 + u * 2048);
      BAR();
      STAMP(2);
      {
        const unsigned n = (unsigned)(bid * 4 + c4) * 64 + l;
        const unsigned off = (n < 6400 ? n : n < 23680 ? n - 6400 : n < 40960 ? n - 23680 : n - 40960) * 128;
        warm(junk, n < 6400 ? off : 0x80000000u, mkrs(p.wo, DM * DM * 2), 0);
        warm(junk, (n >= 6400 && n < 23680) ? off : 0x80000000u, mkrs(p.wg, DM * FF * 2), 0);
        warm(junk, (n >= 23680 && n < 40960) ? off : 0x80000000u, mkrs(p.wu, DM * FF * 2), 0);
        warm(junk, (n >= 40960 && n < 58240) ? off : 0x80000000u, mkrs(p.wd, FF * DM * 2), 0);
      }
      BAR();
      const unsigned kvA = opq(lds + l * 16), kvB = opq(lds + 12 * 5120 + l * 16);
      v4u wf[2][5];
#pragma unroll
      for (int c = 0; c < 5; ++c) wf[0][c] = ldsld<v4u>(kvA + c * 1024);
      const v4u gst = __builtin_amdgcn_raw_buffer_load_b128(rws, (128 * T + 32 * c4 + r32) * 16, WS_ST1, AUX_SC1);
      STAMP(3); CSTAMP(50);
      f4 acc[2][5];
#pragma unroll
      for (int c = 0; c < 5; ++c) { acc[0][c] = f4{0, 0, 0, 0}; acc[1][c] = f4{0, 0, 0, 0}; }
      sfor<0, 20>([&](auto S) {
        constexpr int s = S;
        if constexpr (s == 7) { BAR(); STAMP(25); }
        if constexpr (s == 15) { BAR(); STAMP(26); }
        SCHED();
#pragma unroll
        for (int c = 0; c < 5; ++c) {
          acc[0][c] = mfma16(xf[0][s], wf[s & 1][c], acc[0][c]);
          acc[1][c] = mfma16(xf[1][s], wf[s & 1][c], acc[1][c]);
          SCHED();
          if constexpr (s + 1 < 20) {
            constexpr int n = s + 1;
            wf[n & 1][c] = ldsld<v4u>((n < 12 ? kvA + n * 5120 : kvB + (n - 12) * 5120) + c * 1024);
          }
          SCHED();
        }
      });
      float nmr, rstd;
      {
        v4u g = gst;
        for (unsigned spins = 0; spins < (1u << 16); ++spins) {
          if (__all(g[1] == (unsigned)tag && g[3] == (unsigned)tag)) break;
          __builtin_amdgcn_s_sleep(1);
          g = __builtin_amdgcn_raw_buffer_load_b128(rws, (128 * T + 32 * c4 + r32) * 16, WS_ST1, AUX_SC1);
        }
        asm volatile("s_waitcnt vmcnt(0)" : "+v"(junk));
        nmr = u2f(g[0]); rstd = u2f(g[2]);
        if (hh == 0) { ldsst<float>(lds + STT + c4 * 256 + r32 * 4, nmr); ldsst<float>(lds + STT + c4 * 256 + 128 + r32 * 4, rstd); }
      }
      STAMP(32); CSTAMP(51);
      BAR();
      STAMP(33);
      {
        float cs[5];
#pragma unroll
        for (int c = 0; c < 5; ++c) { const f4 a = ldsld<f4>(lds + CSK + (c * 16 + r16) * 16); cs[c] = (a[0] + a[1]) + (a[2] + a[3]); }
#pragma unroll
        for (int u = 0; u < 2; ++u) {
          const f4 nm = ldsld<f4>(lds + STT + c4 * 256 + (16 * u + 4 * g4) * 4), rs = ldsld<f4>(lds + STT + c4 * 256 + 128 + (16 * u + 4 * g4) * 4);
          const f2 n0 = {nm[0], nm[1]}, n1 = {nm[2], nm[3]}, r0 = {rs[0], rs[1]}, r1 = {rs[2], rs[3]};
          f2 t[5][2];
#pragma unroll
          for (int c = 0; c < 5; ++c) { t[c][0] = f2{acc[u][c][0], acc[u][c][1]} * r0 + n0 * cs[c]; t[c][1] = f2{acc[u][c][2], acc[u][c][3]} * r1 + n1 * cs[c]; }
          const unsigned exw = lds + EX + (2 * c4 + u) * 3072 + l * 8;
#pragma unroll
          for (int c = 0; c < 3; ++c) ldsst<v2u>(exw + c * 512, v2u{packf2(phi2(t[c][0])), packf2(phi2(t[c][1]))});
          v2u v2 = {packf2(t[2][0]), packf2(t[2][1])};
          if (r16 < 8) v2 = r16 < 2 ? v2u{0x3f803f80u, 0x3f803f80u} : v2u{0u, 0u};
          ldsst<v2u>(exw + 3 * 512, v2);
          ldsst<v2u>(exw + 4 * 512, v2u{packf2(t[3][0]), packf2(t[3][1])});
          ldsst<v2u>(exw + 5 * 512, v2u{packf2(t[4][0]), packf2(t[4][1])});
        }
      }
      STAMP(34);
      BAR();
      STAMP(4);
      f4 aq[2][3];
#pragma unroll
      for (int c = 0; c < 3; ++c) { aq[0][c] = f4{0, 0, 0, 0}; aq[1][c] = f4{0, 0, 0, 0}; }
      const unsigned ql = opq(lds + QIMG + l * 16), ql2 = opq(lds + QIMG + 2048 + (g4 * 8 + (r16 & 7)) * 16);
      v4u qf[2][3];
      qf[0][0] = ldsld<v4u>(ql); qf[0][1] = ldsld<v4u>(ql + 1024); qf[0][2] = ldsld<v4u>(ql2);
      sfor<0, 20>([&](auto S) {
        constexpr int s = S;
#pragma unroll
        for (int c = 0; c < 3; ++c) {
          aq[0][c] = mfma16(qf[s & 1][c], xf[0][s], aq[0][c]);
          aq[1][c] = mfma16(qf[s & 1][c], xf[1][s], aq[1][c]);
          SCHED();
          if constexpr (s + 1 < 20) qf[(s + 1) & 1][c] = ldsld<v4u>(c < 2 ? ql + (s + 1) * 2560 + c * 1024 : ql2 + (s + 1) * 2560);
          SCHED();
        }
      });
      STAMP(27);
      {
        v2u P[2][3];
        f4 cs[3];
#pragma unroll
        for (int c = 0; c < 3; ++c) {
          cs[c] = ldsld<f4>(lds + CSQ + (c * 16 + 4 * g4) * 4);
#pragma unroll
          for (int h = 1; h < 4; ++h) cs[c] += ldsld<f4>(lds + CSQ + h * 192 + (c * 16 + 4 * g4) * 4);
        }
#pragma unroll
        for (int u = 0; u < 2; ++u) {
          const float nm = ldsld<float>(lds + STT + c4 * 256 + (16 * u + r16) * 4), rs = ldsld<float>(lds + STT + c4 * 256 + 128 + (16 * u + r16) * 4);
#pragma unroll
          for (int c = 0; c < 3; ++c)
            P[u][c] = v2u{packf2(phi2(f2{aq[u][c][0], aq[u][c][1]} * rs + f2{cs[c][0], cs[c][1]} * nm)), packf2(phi2(f2{aq[u][c][2], aq[u][c][3]} * rs + f2{cs[c][2], cs[c][3]} * nm))};
        }
#pragma unroll
        for (int c = 0; c < 3; ++c) {
          unsigned lo[2], hi[2];
#pragma unroll
          for (int e = 0; e < 2; ++e) {
            const auto a = __builtin_amdgcn_permlane16_swap(P[0][c][e], P[1][c][e], false, false);
            const auto b = __builtin_amdgcn_permlane32_swap(a[0], a[1], false, false);
            lo[e] = b[0]; hi[e] = b[1];
          }
          pq[c] = c < 2 ? v4u{lo[0], lo[1], hi[0], hi[1]} : v4u{lo[0], lo[1], 0u, 0u};
        }
      }
#pragma unroll
      for (int j = 0; j < 8; ++j) srcj[j] = lds + GS + kv_index(j, l) * 16;
      STAMP(6);
    }
  }

  if constexpr (PH & 2) {
    BAR();
    STAMP(8);
    if (wave < 4) {
      v2u af[8];
#pragma unroll
      for (int j = 0; j < 8; ++j) { const f4 t = ldsld<f4>(srcj[j]); af[j] = v2u{pack2(t[0], t[1]), pack2(t[2], t[3])}; }
      f16v e1, e2;
#pragma unroll
      for (int i = 0; i < 16; ++i) { e1[i] = 0.f; e2[i] = 0.f; }
      e1 = mfma32(v4u{af[0].x, af[0].y, af[1].x, af[1].y}, pq[0], e1);
      e2 = mfma32(v4u{af[4].x, af[4].y, af[5].x, af[5].y}, pq[1], e2);
      e1 = mfma32(v4u{af[2].x, af[2].y, af[3].x, af[3].y}, pq[1], e1);
      e2 = mfma32(v4u{af[7].x, af[7].y, 0u, 0u}, pq[2], e2);
      e1 = mfma32(v4u{af[6].x, af[6].y, 0u, 0u}, pq[2], e1);
      const float rA = __builtin_amdgcn_rcpf(__shfl(e1[0], r32) + 1e-6f), rB = __builtin_amdgcn_rcpf(__shfl(e1[1], r32) + 1e-6f);
      v2u o[5];
      auto nrm = [&](float a, float b, float c, float d, float rr) { return v2u{pack2(a * rr, b * rr), pack2(c * rr, d * rr)}; };
      o[0] = nrm(e1[4], e1[5], e1[6], e1[7], rA); o[1] = nrm(e1[8], e1[9], e1[10], e1[11], rA); o[2] = nrm(e1[12], e1[13], e1[14], e1[15], hh ? rB : rA);
      o[3] = nrm(e2[0], e2[1], e2[2], e2[3], rB); o[4] = nrm(e2[4], e2[5], e2[6], e2[7], rB);
      auto off = [&](int i) { const int kq = 10 * hp + 2 * i + hh; return (unsigned)((((4 * T + wave) * 40 + (kq >> 2)) * 64 + (kq & 1) * 32 + r32) * 16 + ((kq >> 1) & 1) * 8); };
      if ((hp & 1) == 0) {
        st16_sc1(v4u{o[0].x, o[0].y, o[1].x, o[1].y}, rws, off(0), WS_OFR); st16_sc1(v4u{o[2].x, o[2].y, o[3].x, o[3].y}, rws, off(2), WS_OFR); st8_sc1(o[4], rws, off(4), WS_OFR);
      } else {
        st8_sc1(o[0], rws, off(0), WS_OFR); st16_sc1(v4u{o[1].x, o[1].y, o[2].x, o[2].y}, rws, off(1), WS_OFR); st16_sc1(v4u{o[3].x, o[3].y, o[4].x, o[4].y}, rws, off(3), WS_OFR);
      }
      STAMP(41);
      WAITV(0);
      if (l == 0) set_flag(ws, WS_F2 + ((4 * T + wave) * 16 + hp) * 128, tag);
      STAMP(9);
    } else if (wave >= 6) {
      __builtin_amdgcn_s_sleep(SL1);
      pf_wo();
    }
  }

  const int tg3 = 3 * wt + wave;
  const bool cw3 = wact && wave < 3 && tg3 < 64;
  {
    v2u xr[8];
    if (cw3) {
      const char* xp = (const char*)p.x + (size_t)(32 * tg3 + r32) * 1280 + wcu * 128 + hh * 8;
#pragma unroll
      for (int i = 0; i < 8; ++i) xr[i] = *(const GAS v2u*)(xp + (i >> 2) * 64 + (i & 3) * 16);
    }
    f16v acc[2];
#pragma unroll
    for (int c = 0; c < 2; ++c)
#pragma unroll
      for (int i = 0; i < 16; ++i) acc[c][i] = 0.f;
    if (cw3) {
      constexpr int WIN = 16;
      v4u of[WIN], wf[2][2];
      const unsigned obase = WS_OFR + tg3 * 40 * 1024;
      Polls p2;
      poll_flags(p2, rws, l < 16 ? WS_F2 + (tg3 * 16 + l) * 128 : NOFLAG, tag);
#pragma unroll
      for (int s = 0; s < 4; ++s) ld_sc1(of[s], l * 16, rws, obase + s * 1024);
      BAR();
      STAMP(10);
      const unsigned w0 = opq(lds + img128_lane(l, 0)), w1 = opq(lds + img128_lane(l, 1));
      const unsigned w0b = opq(lds + 65536 + img128_lane(l, 0)), w1b = opq(lds + 65536 + img128_lane(l, 1));
      wf[0][0] = trfrag(w0, 1024); wf[0][1] = trfrag(w1, 1024);
      sfor<0, 40>([&](auto S) {
        constexpr int s = S, n = s + 1;
        constexpr int hi = s < 12 ? 3 + 2 * s : (s + 15 < 39 ? s + 15 : 39);
        waitv_on<hi - s>(of[s % WIN]);
        if constexpr (s == 0) keep_polls(p2);
        SCHED();
        acc[0] = mfma32(wf[s & 1][0], of[s % WIN], acc[0]);
        SCHED();
        if constexpr (n < 40) wf[n & 1][0] = trfrag(n < 32 ? w0 + n * 2048 : w0b + (n - 32) * 2048, 1024);
        if constexpr (s < 12) ld_sc1(of[(4 + 2 * s) % WIN], l * 16, rws, obase + (4 + 2 * s) * 1024);
        SCHED();
        acc[1] = mfma32(wf[s & 1][1], of[s % WIN], acc[1]);
        SCHED();
        if constexpr (n < 40) wf[n & 1][1] = trfrag(n < 32 ? w1 + n * 2048 : w1b + (n - 32) * 2048, 1024);
        if constexpr (s < 12) ld_sc1(of[(5 + 2 * s) % WIN], l * 16, rws, obase + (5 + 2 * s) * 1024);
        if constexpr (s >= 12 && s + WIN < 40) ld_sc1(of[s % WIN], l * 16, rws, obase + (s + WIN) * 1024);
        SCHED();
      });
    } else {
      if (wave >= 6) WAITV(0);
      BAR();
      if (wave >= 6) {
        __builtin_amdgcn_s_sleep(SL2);
        pf_gu_half2(1);
      }
    }
    BAR();
    STAMP(11);
    if (wave >= 3) {
      __builtin_amdgcn_s_sleep(SL3);
      const rsrc_t rg = mkrs(p.wg, DM * FF * 2), ru = mkrs(p.wu, DM * FF * 2);
      const unsigned v = img128_voff(l, 3456);
#pragma unroll
      for (int i = 0; i < 16; ++i) {
        const int n = 16 * (wave - 3) + i, ks = n >> 2, pc = n & 3;
        glds(lds + ks * 4096 + pc * 1024, v, (pc & 2) ? ru : rg, (ks * 16 + (pc & 1) * 8) * 3456 + gu * 128);
      }
    }
    if (cw3) {
      __builtin_amdgcn_s_setprio(3);
      f2 q1 = {0.f, 0.f}, q2 = {0.f, 0.f};
#pragma unroll
      for (int c = 0; c < 2; ++c) {
        unsigned hp2[8];
#pragma unroll
        for (int i = 0; i < 8; ++i) {
          const unsigned xx = xr[c * 4 + (i >> 1)][i & 1];
          const f2 h = f2{bflo(xx), bfhi(xx)} + f2{acc[c][2 * i], acc[c][2 * i + 1]};
          q1 += h; q2 += h * h;
          hp2[i] = packf2(h);
        }
#pragma unroll
        for (int s = 0; s < 2; ++s) {
          hkeep[2 * c + s] = v4u{hp2[4 * s], hp2[4 * s + 1], hp2[4 * s + 2], hp2[4 * s + 3]};
          st16_sc1(hkeep[2 * c + s], rws, l * 16, WS_HFR + (tg3 * 40 + 4 * wcu + 2 * c + s) * 1024);
        }
      }
      float s1 = q1[0] + q1[1], s2 = q2[0] + q2[1];
      s1 += __shfl_xor(s1, 32); s2 += __shfl_xor(s2, 32);
      if (hh == 0) st8_sc1(v2u{f2u(s1), f2u(s2)}, rws, (32 * tg3 + r32) * 8, WS_HST + wcu * 2048 * 8);
      STAMP(45);
      WAITV(0);
      if (l == 0) set_flag(ws, WS_F3 + (tg3 * 10 + wcu) * 128, tag);
      __builtin_amdgcn_s_setprio(0);
      STAMP(12);
    }
    if (tid == 0 && bid == 0) __hip_atomic_store((gu64*)(ws + WS_EPOCH), tag, __ATOMIC_RELAXED, __HIP_MEMORY_SCOPE_AGENT);
  }

  {
    const int tg = 8 * gtt + wave;
    constexpr int WINH = 12;
    f16v acc[4];
    f2 st[10];
    v4u hf[WINH], wf[2][4];
    const unsigned w0 = opq(lds + img128_lane(l, 0)), w1 = opq(lds + img128_lane(l, 1));
    const unsigned w0b = opq(lds + 65536 + img128_lane(l, 0)), w1b = opq(lds + 65536 + img128_lane(l, 1));
    const unsigned w0c = opq(lds + 131072 + img128_lane(l, 0)), w1c = opq(lds + 131072 + img128_lane(l, 1));
    const unsigned hbase = WS_HFR + tg * 40 * 1024;
    auto ld_w = [&](auto S, auto C) {
      constexpr int s = S, c = C;
      const unsigned a = (c & 1) ? (s < 16 ? w1 + s * 4096 : s < 32 ? w1b + (s - 16) * 4096 : w1c + (s - 32) * 4096)
                                 : (s < 16 ? w0 + s * 4096 : s < 32 ? w0b + (s - 16) * 4096 : w0c + (s - 32) * 4096);
      wf[s & 1][c] = trfrag(a + (c >> 1) * 2048, 1024);
    };
    WAITV(0);
    BAR();
    {
      const unsigned selv = (r16 == (g4 & 1)) ? 0x3f803f80u : 0u;
      const v4u sel = {selv, selv, selv, selv};
      const unsigned c0 = lds + img128_lane(l, 0) + wave * 5 * 4096, c1 = lds + img128_lane(l, 1) + wave * 5 * 4096;
      f4 cs[4];
#pragma unroll
      for (int c = 0; c < 4; ++c) cs[c] = f4{0, 0, 0, 0};
#pragma unroll
      for (int i = 0; i < 5; ++i) {
        const v4u a = trfrag(c0 + i * 4096, 1024), b = trfrag(c1 + i * 4096, 1024), c = trfrag(c0 + i * 4096 + 2048, 1024), d = trfrag(c1 + i * 4096 + 2048, 1024);
        cs[0] = mfma16(sel, a, cs[0]); cs[1] = mfma16(sel, b, cs[1]); cs[2] = mfma16(sel, c, cs[2]); cs[3] = mfma16(sel, d, cs[3]);
      }
      sfor<0, 4>([&](auto C) { ld_w(std::integral_constant<int, 0>{}, C); });
      BAR();
      if (g4 == 0) {
#pragma unroll
        for (int c = 0; c < 4; ++c) {
          ldsst<float>(lds + wave * 512 + (c * 32 + r16) * 4, cs[c][0]);
          ldsst<float>(lds + wave * 512 + (c * 32 + 16 + r16) * 4, cs[c][1]);
        }
      }
      BAR();
      if (tid < 128) {
        float t = 0.f;
#pragma unroll
        for (int w = 0; w < 8; ++w) t += ldsld<float>(lds + w * 512 + tid * 4);
        ldsst<float>(lds + tid * 4, t);
      }
    }
    BAR();
    Polls p3;
    auto gstep = [&](auto S) {
      constexpr int s = S;
      constexpr int hi = s < 8 ? 3 + 2 * s : s == 8 ? 19 : (s + 10 < 39 ? s + 10 : 39);
      constexpr int N = hi - s + (s >= 29 ? 10 : 0);
      waitv_on<N>(hf[s % WINH]);
      if constexpr (s == 0) { keep_polls(p3); STAMP(21); CSTAMP(46); }
      if constexpr (s == 20) STAMP(23);
      SCHED();
      sfor<0, 4>([&](auto C) {
        constexpr int c = C;
        acc[c] = mfma32(wf[s & 1][c], hf[s % WINH], acc[c]);
        SCHED();
        if constexpr (s + 1 < 40) ld_w(std::integral_constant<int, s + 1>{}, C);
        if constexpr (c == 0 && s < 8) ld_sc1(hf[(4 + 2 * s) % WINH], l * 16, rws, hbase + (4 + 2 * s) * 1024);
        if constexpr (c == 2 && s < 8 && 5 + 2 * s < WINH) ld_sc1(hf[(5 + 2 * s) % WINH], l * 16, rws, hbase + (5 + 2 * s) * 1024);
        if constexpr (c == 0 && s >= 9 && s - 1 + WINH < 40) ld_sc1(hf[(s - 1) % WINH], l * 16, rws, hbase + (s - 1 + WINH) * 1024);
        SCHED();
      });
      if constexpr (s < 8 && 5 + 2 * s >= WINH) ld_sc1(hf[(5 + 2 * s) % WINH], l * 16, rws, hbase + (5 + 2 * s) * 1024);
      if constexpr (s == 28) {
#pragma unroll
        for (int c = 0; c < 10; ++c) ld8_sc1(st[c], (32 * tg + r32) * 8, rws, WS_HST + c * 2048 * 8);
      }
      SCHED();
    };
    if (gact) {
      poll_flags(p3, rws, l < 10 ? WS_F3 + (tg * 10 + l) * 128 : NOFLAG, tag);
      STAMP(13);
#pragma unroll
      for (int s = 0; s < 4; ++s) ld_sc1(hf[s], l * 16, rws, hbase + s * 1024);
#pragma unroll
      for (int c = 0; c < 4; ++c)
#pragma unroll
        for (int i = 0; i < 16; ++i) acc[c][i] = 0.f;
      sfor<0, 40>(gstep);
      STAMP(14); CSTAMP(47);
      asm volatile("s_waitcnt vmcnt(0)" : "+v"(st[0]), "+v"(st[1]), "+v"(st[2]), "+v"(st[3]), "+v"(st[4]), "+v"(st[5]), "+v"(st[6]), "+v"(st[7]), "+v"(st[8]), "+v"(st[9]));
      float s1 = 0.f, s2 = 0.f;
#pragma unroll
      for (int c = 0; c < 10; ++c) { s1 += st[c][0]; s2 += st[c][1]; }
      const float mean = s1 * (1.f / 640.f);
      const float rstd = __builtin_amdgcn_rsqf(fmaxf(s2 * (1.f / 640.f) - mean * mean, 0.f) + 1e-5f);
      const float nm = -mean, k0 = -2.3022082f * rstd, k1 = -0.10294324f * rstd * rstd * rstd, ir2 = __builtin_amdgcn_rcpf(rstd * rstd);
      auto epilogue = [&](auto SCALAR) {
#pragma unroll
        for (int c = 0; c < 2; ++c) {
          unsigned ap[8];
#pragma unroll
          for (int i = 0; i < 4; ++i) {
            const unsigned ca = lds + c * 128 + (8 * i + 4 * hh) * 4;
            const f4 cg = ldsld<f4>(ca), cu = ldsld<f4>(ca + 256);
#pragma unroll
            for (int j = 0; j < 2; ++j) {
              if constexpr (SCALAR) {
                float a[2];
#pragma unroll
                for (int e = 0; e < 2; ++e) {
                  float gg = fmaf(nm, cg[2 * j + e], acc[c][4 * i + 2 * j + e]), uu = fmaf(nm, cu[2 * j + e], acc[2 + c][4 * i + 2 * j + e]);
                  asm volatile("" : "+v"(gg), "+v"(uu));
                  float q = fmaf(gg * gg, k1, k0);
                  asm volatile("" : "+v"(q));
                  float d = fmaf(__builtin_amdgcn_exp2f(gg * q), ir2, ir2), pr = gg * uu;
                  asm volatile("" : "+v"(d), "+v"(pr));
                  a[e] = pr * __builtin_amdgcn_rcpf(d);
                  asm volatile("" : "+v"(a[e]));
                }
                ap[2 * i + j] = pack2(a[0], a[1]);
              } else {
                const f2 ag = {acc[c][4 * i + 2 * j], acc[c][4 * i + 2 * j + 1]}, au = {acc[2 + c][4 * i + 2 * j], acc[2 + c][4 * i + 2 * j + 1]};
                const f2 kg = {cg[2 * j], cg[2 * j + 1]}, ku = {cu[2 * j], cu[2 * j + 1]};
                const f2 gg = ag + nm * kg, uu = au + nm * ku;
                const f2 t = gg * (gg * gg * k1 + k0);
                const f2 d = f2{__builtin_amdgcn_exp2f(t[0]), __builtin_amdgcn_exp2f(t[1])} * ir2 + ir2;
                const f2 a = (gg * uu) * f2{__builtin_amdgcn_rcpf(d[0]), __builtin_amdgcn_rcpf(d[1])};
                ap[2 * i + j] = pack2(a[0], a[1]);
              }
            }
          }
#pragma unroll
          for (int s = 0; s < 2; ++s)
            st16_sc1(v4u{ap[4 * s], ap[4 * s + 1], ap[4 * s + 2], ap[4 * s + 3]}, rws, l * 16, WS_AFR + tg * AFR_TG + (4 * gu + 2 * c + s) * 1024);
        }
      };
      if (wave < 4) epilogue(std::true_type{}); else epilogue(std::false_type{});
      STAMP(15);
      WAITV(0);
      if (l == 0) set_flag(ws, WS_F4 + (tg * 27 + gu) * 128, tag);
    }
    BAR();
    STAMP(16);
  }

  {
    const rsrc_t rd = mkrs(p.wd, FF * DM * 2);
    const unsigned vw = img128_voff(l, 1280);
    auto dma = [&](int k2, int j, int slotj, int pc) { glds(lds + slotj * 2048 + pc * 1024, vw, rd, ((54 * k2 + j) * 16 + pc * 8) * 1280 + wcu * 128); };
    const int tgl = wave % 3, kh = wave / 3, tg = 3 * wt + tgl;
    const bool cw = wact && wave < 6 && tg < 64;
#pragma unroll
    for (int i = 0; i < 20; ++i) {
      const int n = 80 * (i / 10) + 10 * wave + (i % 10), part = n / 80, k2 = (n % 80) / 40, j = 20 * part + (n % 40) / 2;
      dma(k2, j, 40 * part + 20 * k2 + (j - 20 * part), n & 1);
    }
    f16v acc[2];
#pragma unroll
    for (int c = 0; c < 2; ++c)
#pragma unroll
      for (int i = 0; i < 16; ++i) acc[c][i] = 0.f;
    constexpr unsigned RED = 0, OUTT = 57344;
    if (cw) {
#define DWIN 24
      constexpr int WIN = DWIN;
      v4u af[WIN], wf[2][2];
      const unsigned abase = WS_AFR + tg * AFR_TG + 54 * kh * 1024;
      const unsigned w0 = opq(lds + img128_lane(l, 0) + kh * 20 * 2048), w1 = opq(lds + img128_lane(l, 1) + kh * 20 * 2048);
      const unsigned w0b = opq(lds + 81920 + img128_lane(l, 0) + kh * 20 * 2048), w1b = opq(lds + 81920 + img128_lane(l, 1) + kh * 20 * 2048);
      auto ldw = [&](auto S, auto C) {
        constexpr int s = S, c = C;
        if constexpr (c == 0) wf[s & 1][0] = trfrag(s < 20 ? w0 + s * 2048 : s < 40 ? w0b + (s - 20) * 2048 : w0 + (s - 40) * 2048, 1024);
        else wf[s & 1][1] = trfrag(s < 20 ? w1 + s * 2048 : s < 40 ? w1b + (s - 20) * 2048 : w1 + (s - 40) * 2048, 1024);
      };
      using I0 = std::integral_constant<int, 0>;
      using I1 = std::integral_constant<int, 1>;
      Polls p4;
      auto step = [&](auto S) {
        constexpr int s = S;
        constexpr int hi = s + WIN - 1 < 53 ? s + WIN - 1 : 53;
        waitv_on<hi - s>(af[s % WIN]);
        if constexpr (s == 0) { keep_polls(p4); STAMP(24); }
        SCHED();
        acc[0] = mfma32(wf[s & 1][0], af[s % WIN], acc[0]);
        SCHED();
        if constexpr (s + 1 < 54) ldw(std::integral_constant<int, s + 1>{}, I0{});
        SCHED();
        acc[1] = mfma32(wf[s & 1][1], af[s % WIN], acc[1]);
        SCHED();
        if constexpr (s + 1 < 54) ldw(std::integral_constant<int, s + 1>{}, I1{});
        if constexpr (s + WIN < 54) ld_sc1(af[s % WIN], l * 16, rws, abase + (s + WIN) * 1024);
        SCHED();
      };
      poll_flags(p4, rws, l < 27 ? WS_F4 + (tg * 27 + l) * 128 : NOFLAG, tag);
      STAMP(17); CSTAMP(48);
#pragma unroll
      for (int s = 0; s < 4; ++s) ld_sc1(af[s], l * 16, rws, abase + s * 1024);
      BAR();
      STAMP(22);
#pragma unroll
      for (int s = 4; s < WIN; ++s) ld_sc1(af[s], l * 16, rws, abase + s * 1024);
      ldw(I0{}, I0{}); ldw(I0{}, I1{});
      sfor<0, 19>(step);
      BAR();
      STAMP(18);
      sfor<19, 39>(step);
      BAR();
      sfor<39, 54>(step);
      STAMP(19); CSTAMP(49);
      BAR();
      if (kh == 1) {
#pragma unroll
        for (int c = 0; c < 2; ++c)
#pragma unroll
          for (int i = 0; i < 4; ++i)
            ldsst<f4>(lds + RED + tgl * 8192 + (c * 4 + i) * 1024 + l * 16, f4{acc[c][4 * i], acc[c][4 * i + 1], acc[c][4 * i + 2], acc[c][4 * i + 3]});
      }
      BAR();
      if (kh == 0) {
        const unsigned ot = lds + OUTT + tgl * 8192;
#pragma unroll
        for (int c = 0; c < 2; ++c)
#pragma unroll
          for (int i = 0; i < 4; ++i) {
            const f4 o = ldsld<f4>(lds + RED + tgl * 8192 + (c * 4 + i) * 1024 + l * 16);
            const unsigned h01 = hkeep[2 * c + (i >> 1)][2 * (i & 1)], h23 = hkeep[2 * c + (i >> 1)][2 * (i & 1) + 1];
            const f2 oa = f2{bflo(h01), bfhi(h01)} + (f2{acc[c][4 * i], acc[c][4 * i + 1]} + f2{o[0], o[1]});
            const f2 ob = f2{bflo(h23), bfhi(h23)} + (f2{acc[c][4 * i + 2], acc[c][4 * i + 3]} + f2{o[2], o[3]});
            ldsst<v2u>(ot + r32 * 144 + c * 64 + i * 16 + hh * 8, v2u{packf2(oa), packf2(ob)});
          }
        WAITL0();
        const rsrc_t ro = mkrs(p.out, SEQ * DM * 2);
#pragma unroll
        for (int i = 0; i < 4; ++i) {
          const int row = 8 * i + (l >> 3);
          st16_sc1(ldsld<v4u>(ot + row * 144 + (l & 7) * 16), ro, (32 * tg + row) * 1280 + wcu * 128 + (l & 7) * 16, 0);
        }
      }
    } else {
      WAITV(0);
      BAR();
      BAR();
      if (wave >= 6) {
#pragma unroll
        for (int j = 40; j < 54; ++j) { dma(wave - 6, j, 20 * (wave - 6) + (j - 40), 0); dma(wave - 6, j, 20 * (wave - 6) + (j - 40), 1); }
        WAITV(0);
      }
      BAR();
      BAR();
      BAR();
    }
  }
}

extern "C" void kernel_launch(void* const* d_in, const int* in_sizes, int n_in, void* d_out, int out_size, void* d_ws, size_t ws_size,
                              hipStream_t stream) {
  Params p;
  p.x = d_in[0]; p.wqkv = d_in[1]; p.wo = d_in[2]; p.wg = d_in[4]; p.wu = d_in[5]; p.wd = d_in[6]; p.out = d_out; p.ws = (char*)d_ws;
  (void)hipFuncSetAttribute((const void*)blk_kernel<31>, hipFuncAttributeMaxDynamicSharedMemorySize, LDS_BYTES);
  blk_kernel<31><<<256, 512, LDS_BYTES, stream>>>(p);
}
```
